```python
import torch

def _softmax(x, axis=-1):
    return torch.softmax(x, dim=axis)
def _gelu(x):
    return torch.nn.functional.gelu(x, approximate="tanh")
def _silu(x):
    return torch.nn.functional.silu(x)
def _sigmoid(x):
    return torch.sigmoid(x)
def _layernorm(x, axis=-1, eps=1e-5):
    m = x.mean(dim=axis, keepdim=True)
    v = x.var(dim=axis, keepdim=True, unbiased=False)
    return (x - m) / torch.sqrt(v + eps)
def _scatter_add(tgt, idx, src, axis=0):
    return tgt.clone().index_add_(axis, idx, src)
def _segment_sum(src, seg, n):
    out = torch.zeros((n,) + src.shape[1:], dtype=src.dtype)
    return out.index_add_(0, seg, src)
def _pad(x, pw):
    flat = tuple(v for pair in reversed(pw) for v in pair)
    return torch.nn.functional.pad(x, flat)

kernel_name = 'standalone__spectral_conv_chebnet__graph'

def setup_inputs():

    _w = (lambda a: (lambda t: t.to(torch.bfloat16) if t.is_floating_point() else t)(torch.as_tensor(a)))
    N, deg, D, K = 50000, 16, 64, 2
    E = N * deg
    src = (lambda lo, hi, *s: torch.randint(lo, hi, s))(0, N, E).to(torch.int64)
    dst = (lambda lo, hi, *s: torch.randint(lo, hi, s))(0, N, E).to(torch.int64)
    deg_cnt = torch.bincount(dst, minlength=N).to(float) + 1.0
    dis = (1.0 / (lambda _x: torch.sqrt(torch.as_tensor(_x)))(deg_cnt)).to(float)
    ew = -(dis[src] * dis[dst])
    return {
        "x": _w((lambda *s: torch.randn(*s, dtype=torch.bfloat16) * 0.05)(N, D)),
        "src": _w(src),
        "dst": _w(dst),
        "ew": _w(ew),
        "w": _w((lambda *s: torch.randn(*s, dtype=torch.bfloat16) * 0.05)(K, D, D) * 0.2),
    }

def reference(x, src, dst, ew, w):
    N, K = x.shape[0], w.shape[0]
    Ts = [x]
    if K > 1:
        Ts.append(_segment_sum(ew[:, None] * x[src], dst, N))
    for k in range(2, K):
        Tk = 2.0 * _segment_sum(ew[:, None] * Ts[-1][src], dst, N) - Ts[-2]
        Ts.append(Tk)
    T = torch.stack(Ts, 0)
    return torch.einsum('knd,kde->ne', T, w)
```

```cpp
#include <hip/hip_runtime.h>
#include <hip/hip_bf16.h>
#include <stdint.h>


typedef __attribute__((ext_vector_type(8))) short bf16x8;
typedef __attribute__((ext_vector_type(4))) float f32x4;
typedef unsigned long long u64;
typedef unsigned int u32;

#define D 64
#define A_THREADS 1024
#define A_EPT 4
#define A_CHUNK_MAX (A_THREADS * A_EPT)
#define MAX_BUCKETS 1024
#define B_THREADS 512
#define B_GROUPS (B_THREADS / 8)
#define B_CAP 2048
#define B_RPT (B_CAP / B_THREADS)
#define MAX_NPB 128
#define MAX_CHUNKS 2048
#define ACC_STRIDE 68

__device__ __forceinline__ float bf_lo(u32 v) { return __uint_as_float(v << 16); }
__device__ __forceinline__ float bf_hi(u32 v) { return __uint_as_float(v & 0xffff0000u); }
__device__ __forceinline__ u32 pack_bf16(float a, float b) {
    __hip_bfloat16 ha = __float2bfloat16(a), hb = __float2bfloat16(b);
    return (u32)__builtin_bit_cast(unsigned short, ha) | ((u32)__builtin_bit_cast(unsigned short, hb) << 16);
}

__device__ __forceinline__ u32 wave_incl_scan(u32 v, int lane) {
#pragma unroll
    for (int o = 1; o < 64; o <<= 1) {
        u32 t = __shfl_up(v, o, 64);
        if (lane >= o) v += t;
    }
    return v;
}

__global__ __launch_bounds__(A_THREADS) void bin_kernel(
    const int* __restrict__ src, const int* __restrict__ dst, const unsigned short* __restrict__ ew,
    const unsigned short* __restrict__ w, int E, int chunk, int npb, u32 magic, int nBuckets, int nChunks,
    u64* __restrict__ recs, u32* __restrict__ offT, unsigned short* __restrict__ wfrag)
{
    __shared__ u32 hist[MAX_BUCKETS];
    __shared__ u32 wsum[A_THREADS / 64];
    __shared__ __attribute__((aligned(16))) u64 stage[A_CHUNK_MAX];

    const int tid = threadIdx.x, lane = tid & 63, wv = tid >> 6;
    const int c = blockIdx.x;
    const int e0 = c * chunk;
    const int cnt = min(chunk, E - e0);

    if (c == 0) {
        const int l = tid & 63, sc = tid >> 6, s = sc >> 2, ct = sc & 3;
        const int r = l & 15, q = l >> 4;
        const int f = 16 * (r >> 2) + 4 * ct + (r & 3);
        unsigned short v[8];
#pragma unroll
        for (int j = 0; j < 8; ++j) v[j] = w[(32 * s + 8 * q + j) * D + f];
        uint4 o;
        o.x = v[0] | ((u32)v[1] << 16); o.y = v[2] | ((u32)v[3] << 16);
        o.z = v[4] | ((u32)v[5] << 16); o.w = v[6] | ((u32)v[7] << 16);
        ((uint4*)wfrag)[tid] = o;
    }

    hist[tid] = 0;
    int d[A_EPT], s[A_EPT]; u32 wgt[A_EPT];
    const int i0 = tid * A_EPT;
    if (i0 + A_EPT <= cnt) {
        int4 dv = *(const int4*)(dst + e0 + i0);
        int4 sv = *(const int4*)(src + e0 + i0);
        uint2 wv2 = *(const uint2*)(ew + e0 + i0);
        d[0] = dv.x; d[1] = dv.y; d[2] = dv.z; d[3] = dv.w;
        s[0] = sv.x; s[1] = sv.y; s[2] = sv.z; s[3] = sv.w;
        wgt[0] = wv2.x & 0xffffu; wgt[1] = wv2.x >> 16; wgt[2] = wv2.y & 0xffffu; wgt[3] = wv2.y >> 16;
    } else {
#pragma unroll
        for (int k = 0; k < A_EPT; ++k) {
            if (i0 + k < cnt) { d[k] = dst[e0 + i0 + k]; s[k] = src[e0 + i0 + k]; wgt[k] = ew[e0 + i0 + k]; }
            else { d[k] = -1; s[k] = 0; wgt[k] = 0; }
        }
    }
    __syncthreads();
    int bk[A_EPT]; u32 rank[A_EPT]; u32 hi[A_EPT];
#pragma unroll
    for (int k = 0; k < A_EPT; ++k) {
        if (d[k] >= 0) {
            u32 q = __umulhi((u32)d[k], magic);
            int r = d[k] - (int)q * npb;
            if (r < 0) { q -= 1; r += npb; }
            if (r >= npb) { q += 1; r -= npb; }
            bk[k] = (int)q;
            hi[k] = ((u32)r << 16) | wgt[k];
            rank[k] = atomicAdd(&hist[q], 1u);
        } else { bk[k] = -1; hi[k] = 0; rank[k] = 0; }
    }
    __syncthreads();
    const u32 h = hist[tid];
    u32 inc = wave_incl_scan(h, lane);
    if (lane == 63) wsum[wv] = inc;
    __syncthreads();
    if (wv == 0) {
        u32 t = lane < (A_THREADS / 64) ? wsum[lane] : 0;
        u32 ti = wave_incl_scan(t, lane);
        if (lane < (A_THREADS / 64)) wsum[lane] = ti - t;
    }
    __syncthreads();
    const u32 base = inc - h + wsum[wv];
    hist[tid] = base;
    if (tid < nBuckets) offT[(size_t)tid * nChunks + c] = base | (h << 16);
    __syncthreads();
#pragma unroll
    for (int k = 0; k < A_EPT; ++k) {
        if (bk[k] >= 0) stage[hist[bk[k]] + rank[k]] = ((u64)hi[k] << 32) | (u32)s[k];
    }
    __syncthreads();
    u64* outp = recs + (size_t)c * chunk;
    for (int i = tid * 2; i < cnt; i += A_THREADS * 2) {
        if (i + 1 < cnt) *(uint4*)(outp + i) = *(const uint4*)(stage + i);
        else outp[i] = stage[i];
    }
}

__global__ __launch_bounds__(B_THREADS) void gather_kernel(
    const unsigned short* __restrict__ x, const u64* __restrict__ recs, const u32* __restrict__ offT,
    const uint4* __restrict__ wfrag, unsigned short* __restrict__ out,
    int N, int npb, int nChunks, int chunk)
{
    __shared__ __attribute__((aligned(16))) float acc[MAX_NPB * ACC_STRIDE];
    __shared__ __attribute__((aligned(16))) u64 srec[B_CAP];
    __shared__ u32 segPre[MAX_CHUNKS + 1];
    __shared__ u32 segStart[MAX_CHUNKS];
    __shared__ u32 ncnt[MAX_NPB];
    __shared__ u32 wsum[B_THREADS / 64];
    __shared__ u32 s_carry;

    const int tid = threadIdx.x, lane = tid & 63, wv = tid >> 6;
    const int b = blockIdx.x;
    const int node0 = b * npb;
    const int nn = min(npb, N - node0);

    for (int i = tid; i < MAX_NPB * ACC_STRIDE; i += B_THREADS) acc[i] = 0.f;
    if (tid == 0) s_carry = 0;
    __syncthreads();
    for (int c0 = 0; c0 < nChunks; c0 += B_THREADS) {
        const int c = c0 + tid;
        u32 v = 0;
        if (c < nChunks) v = offT[(size_t)b * nChunks + c];
        const u32 len = v >> 16;
        u32 inc = wave_incl_scan(len, lane);
        if (lane == 63) wsum[wv] = inc;
        __syncthreads();
        u32 wbase = s_carry;
        for (int k = 0; k < wv; ++k) wbase += wsum[k];
        if (c < nChunks) {
            segPre[c] = wbase + inc - len;
            segStart[c] = (u32)c * (u32)chunk + (v & 0xffffu);
        }
        __syncthreads();
        if (tid == B_THREADS - 1) s_carry = wbase + inc;
        __syncthreads();
    }
    const u32 M = s_carry;
    if (tid == 0) segPre[nChunks] = M;
    __syncthreads();

    const int g = tid >> 3, j = tid & 7;
    const unsigned short* xj = x + j * 8;

    for (u32 t0 = 0; t0 < M; t0 += B_CAP) {
        const u32 Mt = min((u32)B_CAP, M - t0);
        if (tid < MAX_NPB) ncnt[tid] = 0;
        u64 r[B_RPT];
#pragma unroll
        for (int k = 0; k < B_RPT; ++k) {
            const u32 i = t0 + k * B_THREADS + tid;
            r[k] = ~0ull;
            if (k * B_THREADS + tid < Mt) {
                int lo = 0, hi = nChunks;
                while (hi - lo > 1) {
                    int mid = (lo + hi) >> 1;
                    if (segPre[mid] <= i) lo = mid; else hi = mid;
                }
                r[k] = recs[segStart[lo] + (i - segPre[lo])];
            }
        }
        __syncthreads();
        u32 rank[B_RPT];
#pragma unroll
        for (int k = 0; k < B_RPT; ++k) {
            if (k * B_THREADS + tid < Mt) rank[k] = atomicAdd(&ncnt[(u32)(r[k] >> 48)], 1u);
        }
        __syncthreads();
        if (wv == 0) {
            u32 a0 = ncnt[2 * lane], a1 = ncnt[2 * lane + 1];
            u32 inc = wave_incl_scan(a0 + a1, lane);
            u32 ex = inc - a0 - a1;
            ncnt[2 * lane] = ex; ncnt[2 * lane + 1] = ex + a0;
        }
        __syncthreads();
#pragma unroll
        for (int k = 0; k < B_RPT; ++k) {
            if (k * B_THREADS + tid < Mt) srec[ncnt[(u32)(r[k] >> 48)] + rank[k]] = r[k];
        }
        __syncthreads();
        const u32 beg = (u32)(((u64)g * Mt) / B_GROUPS), end = (u32)(((u64)(g + 1) * Mt) / B_GROUPS);
        if (beg < end) {
            float a[8];
#pragma unroll
            for (int k = 0; k < 8; ++k) a[k] = 0.f;
            u32 cur = (u32)(srec[beg] >> 48);
            for (u32 i = beg; i < end; i += 4) {
                u64 rr[4]; uint4 v[4];
#pragma unroll
                for (int k = 0; k < 4; ++k) {
                    rr[k] = srec[min(i + k, end - 1)];
                    v[k] = *(const uint4*)(xj + (size_t)(u32)rr[k] * D);
                }
#pragma unroll
                for (int k = 0; k < 4; ++k) {
                    if (i + k < end) {
                        const u32 hiw = (u32)(rr[k] >> 32);
                        const u32 nd = hiw >> 16;
                        if (nd != cur) {
                            float* ap = acc + cur * ACC_STRIDE + j * 8;
#pragma unroll
                            for (int q = 0; q < 8; ++q) { atomicAdd(ap + q, a[q]); a[q] = 0.f; }
                            cur = nd;
                        }
                        const float wgt = bf_lo(hiw);
                        a[0] = fmaf(wgt, bf_lo(v[k].x), a[0]); a[1] = fmaf(wgt, bf_hi(v[k].x), a[1]);
                        a[2] = fmaf(wgt, bf_lo(v[k].y), a[2]); a[3] = fmaf(wgt, bf_hi(v[k].y), a[3]);
                        a[4] = fmaf(wgt, bf_lo(v[k].z), a[4]); a[5] = fmaf(wgt, bf_hi(v[k].z), a[5]);
                        a[6] = fmaf(wgt, bf_lo(v[k].w), a[6]); a[7] = fmaf(wgt, bf_hi(v[k].w), a[7]);
                    }
                }
            }
            float* ap = acc + cur * ACC_STRIDE + j * 8;
#pragma unroll
            for (int q = 0; q < 8; ++q) atomicAdd(ap + q, a[q]);
        }
        __syncthreads();
    }

    for (int t = wv; t * 16 < nn; t += B_THREADS / 64) {
        const int nl = t * 16 + (lane & 15), q = lane >> 4;
        const bool ok = nl < nn;
        bf16x8 bfr[4];
        {
            uint4 z = make_uint4(0, 0, 0, 0);
            uint4 x0 = z, x1 = z, t0 = z, t1 = z;
            if (ok) {
                const unsigned short* xr = x + (size_t)(node0 + nl) * D + 8 * q;
                x0 = *(const uint4*)(xr);
                x1 = *(const uint4*)(xr + 32);
                const float* ar = acc + nl * ACC_STRIDE + 8 * q;
                f32x4 f0 = *(const f32x4*)(ar), f1 = *(const f32x4*)(ar + 4);
                f32x4 f2 = *(const f32x4*)(ar + 32), f3 = *(const f32x4*)(ar + 36);
                t0 = make_uint4(pack_bf16(f0[0], f0[1]), pack_bf16(f0[2], f0[3]), pack_bf16(f1[0], f1[1]), pack_bf16(f1[2], f1[3]));
                t1 = make_uint4(pack_bf16(f2[0], f2[1]), pack_bf16(f2[2], f2[3]), pack_bf16(f3[0], f3[1]), pack_bf16(f3[2], f3[3]));
            }
            bfr[0] = __builtin_bit_cast(bf16x8, x0); bfr[1] = __builtin_bit_cast(bf16x8, x1);
            bfr[2] = __builtin_bit_cast(bf16x8, t0); bfr[3] = __builtin_bit_cast(bf16x8, t1);
        }
        u32 o[8];
#pragma unroll
        for (int ct = 0; ct < 4; ++ct) {
            f32x4 c4 = {0.f, 0.f, 0.f, 0.f};
#pragma unroll
            for (int s = 0; s < 4; ++s) {
                bf16x8 af = __builtin_bit_cast(bf16x8, wfrag[(s * 4 + ct) * 64 + lane]);
                c4 = __builtin_amdgcn_mfma_f32_16x16x32_bf16(af, bfr[s], c4, 0, 0, 0);
            }
            o[2 * ct] = pack_bf16(c4[0], c4[1]);
            o[2 * ct + 1] = pack_bf16(c4[2], c4[3]);
        }
        if (ok) {
            uint4* op = (uint4*)(out + (size_t)(node0 + nl) * D + 16 * q);
            op[0] = make_uint4(o[0], o[1], o[2], o[3]);
            op[1] = make_uint4(o[4], o[5], o[6], o[7]);
        }
    }
}

extern "C" void kernel_launch(void* const* d_in, const int* in_sizes, int n_in,
                              void* d_out, int out_size, void* d_ws, size_t ws_size,
                              hipStream_t stream) {
    const unsigned short* x = (const unsigned short*)d_in[0];
    const int* src = (const int*)d_in[1];
    const int* dst = (const int*)d_in[2];
    const unsigned short* ew = (const unsigned short*)d_in[3];
    const unsigned short* w = (const unsigned short*)d_in[4];
    unsigned short* out = (unsigned short*)d_out;
    const int N = in_sizes[0] / D;
    const int E = in_sizes[1];

    int nBucketsTarget = 512;
    int npb = (N + nBucketsTarget - 1) / nBucketsTarget;
    if (npb > MAX_NPB) npb = MAX_NPB;
    if (npb < 1) npb = 1;
    const int nBuckets = (N + npb - 1) / npb;
    const u32 magic = (u32)((0x100000000ull / (u64)npb) + 1ull);

    int nChunks = 256;
    int chunk = (E + nChunks - 1) / nChunks;
    chunk = (chunk + 3) & ~3;
    if (chunk > A_CHUNK_MAX) chunk = A_CHUNK_MAX;
    if (chunk < 4) chunk = 4;
    nChunks = (E + chunk - 1) / chunk;
    if (nChunks < 1) nChunks = 1;

    char* ws = (char*)d_ws;
    unsigned short* wfrag = (unsigned short*)ws;
    u32* offT = (u32*)(ws + 16384);
    size_t offBytes = ((size_t)nBuckets * nChunks * 4 + 255) & ~(size_t)255;
    u64* recs = (u64*)(ws + 16384 + offBytes);

    bin_kernel<<<nChunks, A_THREADS, 0, stream>>>(src, dst, ew, w, E, chunk, npb, magic, nBuckets, nChunks,
                                                  recs, offT, wfrag);
    gather_kernel<<<nBuckets, B_THREADS, 0, stream>>>(x, recs, offT, (const uint4*)wfrag, out, N, npb, nChunks, chunk);
}
```

```cpp
#include <hip/hip_runtime.h>
#include <hip/hip_bf16.h>
#include <stdint.h>


typedef __attribute__((ext_vector_type(8))) short bf16x8;
typedef __attribute__((ext_vector_type(4))) float f32x4;
typedef unsigned long long u64;
typedef unsigned int u32;

#define D 64
#define A_THREADS 1024
#define A_EPT 4
#define A_CHUNK_MAX (A_THREADS * A_EPT)
#define MAX_BUCKETS 512
#define MAX_CHUNKS 256
#define B_THREADS 512
#define MAX_NPB 128
#define B_TILES (MAX_NPB / 16)
#define B_RPT 4
#define B_CAP (B_THREADS * B_RPT)
#define B_SCAP (B_CAP + 32 * B_TILES)
#define ACC_STRIDE 68

__device__ __forceinline__ u32 pack_bf16(float a, float b) {
    __hip_bfloat16 ha = __float2bfloat16(a), hb = __float2bfloat16(b);
    return (u32)__builtin_bit_cast(unsigned short, ha) | ((u32)__builtin_bit_cast(unsigned short, hb) << 16);
}

__device__ __forceinline__ u32 wave_incl_scan(u32 v) {
    v += (u32)__builtin_amdgcn_update_dpp(0, (int)v, 0x111, 0xf, 0xf, false);
    v += (u32)__builtin_amdgcn_update_dpp(0, (int)v, 0x112, 0xf, 0xf, false);
    v += (u32)__builtin_amdgcn_update_dpp(0, (int)v, 0x114, 0xf, 0xf, false);
    v += (u32)__builtin_amdgcn_update_dpp(0, (int)v, 0x118, 0xf, 0xf, false);
    v += (u32)__builtin_amdgcn_update_dpp(0, (int)v, 0x142, 0xa, 0xf, false);
    v += (u32)__builtin_amdgcn_update_dpp(0, (int)v, 0x143, 0xc, 0xf, false);
    return v;
}

__device__ __forceinline__ u32 pk_sub_u16(u32 a, u32 b) {
    typedef __attribute__((ext_vector_type(2))) unsigned short us2;
    us2 r = __builtin_bit_cast(us2, a) - __builtin_bit_cast(us2, b);
    return __builtin_bit_cast(u32, r);
}
__device__ __forceinline__ u32 pk_zero_mask(u32 d) {
    typedef __attribute__((ext_vector_type(2))) unsigned short us2;
    us2 dv = __builtin_bit_cast(us2, d);
    us2 one = {1, 1};
    us2 m = __builtin_elementwise_min(dv, one) - one;
    return __builtin_bit_cast(u32, m);
}

__global__ __launch_bounds__(A_THREADS) void bin_kernel(
    const int* __restrict__ src, const int* __restrict__ dst, const unsigned short* __restrict__ ew,
    const unsigned short* __restrict__ w, int E, int chunk, int npb, u32 magic, int nBuckets, int nChunks,
    u64* __restrict__ recs, u32* __restrict__ offT, unsigned short* __restrict__ wfrag)
{
    __shared__ u32 hist[MAX_BUCKETS];
    __shared__ u32 hbase[MAX_BUCKETS];
    __shared__ u32 wsum[MAX_BUCKETS / 64];
    __shared__ __attribute__((aligned(16))) u64 stage[A_CHUNK_MAX];

    const int tid = threadIdx.x, lane = tid & 63, wv = tid >> 6;
    const int c = blockIdx.x;
    const int e0 = c * chunk;
    const int cnt = min(chunk, E - e0);

    int d[A_EPT], s[A_EPT]; u32 wgt[A_EPT];
    const int i0 = tid * A_EPT;
    if (i0 + A_EPT <= cnt) {
        int4 dv = *(const int4*)(dst + e0 + i0);
        int4 sv = *(const int4*)(src + e0 + i0);
        uint2 wv2 = *(const uint2*)(ew + e0 + i0);
        d[0] = dv.x; d[1] = dv.y; d[2] = dv.z; d[3] = dv.w;
        s[0] = sv.x; s[1] = sv.y; s[2] = sv.z; s[3] = sv.w;
        wgt[0] = wv2.x & 0xffffu; wgt[1] = wv2.x >> 16; wgt[2] = wv2.y & 0xffffu; wgt[3] = wv2.y >> 16;
    } else {
#pragma unroll
        for (int k = 0; k < A_EPT; ++k) {
            if (i0 + k < cnt) { d[k] = dst[e0 + i0 + k]; s[k] = src[e0 + i0 + k]; wgt[k] = ew[e0 + i0 + k]; }
            else { d[k] = -1; s[k] = 0; wgt[k] = 0; }
        }
    }
    if (tid < MAX_BUCKETS) hist[tid] = 0;

    if (c == 0) {
        const int l = tid & 63, sc = tid >> 6, ks = sc >> 2, ct = sc & 3;
        const int r = l & 15, q = l >> 4;
        const int f = 16 * (r >> 2) + 4 * ct + (r & 3);
        unsigned short v[8];
#pragma unroll
        for (int j = 0; j < 8; ++j) {
            const int cc = 4 * q + 2 * (ks - 2) + (j >> 2);
            const int krow = (ks < 2) ? (32 * ks + 8 * q + j) : (64 + 8 * (cc & 7) + 4 * (cc >> 3) + (j & 3));
            v[j] = w[krow * D + f];
        }
        uint4 o;
        o.x = v[0] | ((u32)v[1] << 16); o.y = v[2] | ((u32)v[3] << 16);
        o.z = v[4] | ((u32)v[5] << 16); o.w = v[6] | ((u32)v[7] << 16);
        ((uint4*)wfrag)[tid] = o;
    }
    __syncthreads();
    int bk[A_EPT]; u32 rank[A_EPT]; u32 hi[A_EPT];
#pragma unroll
    for (int k = 0; k < A_EPT; ++k) {
        bk[k] = -1; hi[k] = 0; rank[k] = 0;
        if (d[k] >= 0) {
            u32 q = __umulhi((u32)d[k], magic);
            int r = d[k] - (int)q * npb;
            if (r < 0) { q -= 1; r += npb; }
            if (r >= npb) { q += 1; r -= npb; }
            bk[k] = (int)q;
            hi[k] = ((u32)r << 16) | wgt[k];
            rank[k] = atomicAdd(&hist[q], 1u);
        }
    }
    __syncthreads();
    u32 h = 0, inc = 0;
    if (tid < MAX_BUCKETS) {
        h = hist[tid];
        inc = wave_incl_scan(h);
        if (lane == 63) wsum[wv] = inc;
    }
    __syncthreads();
    if (tid < MAX_BUCKETS) {
        u32 wb = 0;
#pragma unroll
        for (int k = 0; k < MAX_BUCKETS / 64; ++k) { const u32 t = wsum[k]; if (k < wv) wb += t; }
        const u32 base = inc - h + wb;
        hbase[tid] = base;
        if (tid < nBuckets) offT[(size_t)tid * nChunks + c] = base | (h << 16);
    }
    __syncthreads();
#pragma unroll
    for (int k = 0; k < A_EPT; ++k) {
        if (bk[k] >= 0) stage[hbase[bk[k]] + rank[k]] = ((u64)hi[k] << 32) | (u32)s[k];
    }
    __syncthreads();
    u64* outp = recs + (size_t)c * chunk;
    for (int i = tid * 2; i < cnt; i += A_THREADS * 2) {
        if (i + 1 < cnt) *(uint4*)(outp + i) = *(const uint4*)(stage + i);
        else outp[i] = stage[i];
    }
}

__global__ __launch_bounds__(B_THREADS) void gather_kernel(
    const unsigned short* __restrict__ x, const u64* __restrict__ recs, const u32* __restrict__ offT,
    const uint4* __restrict__ wfrag, unsigned short* __restrict__ out,
    int N, int npb, int nChunks, int chunk)
{
    __shared__ __attribute__((aligned(16))) u32 sSrc[B_SCAP];
    __shared__ __attribute__((aligned(16))) unsigned short sNode[B_SCAP];
    __shared__ __attribute__((aligned(16))) unsigned short sEw[B_SCAP];
    __shared__ u32 sIdx[B_CAP];
    __shared__ u32 ncnt[MAX_NPB];
    __shared__ u32 nbase[MAX_NPB];
    __shared__ u32 wsum[B_THREADS / 64];

    const int tid = threadIdx.x, lane = tid & 63, wv = __builtin_amdgcn_readfirstlane(tid >> 6);
    const int nB = gridDim.x, xq = nB >> 3, xr = nB & 7, xcd = blockIdx.x & 7;
    const int b = (xcd < xr ? xcd * (xq + 1) : xr * (xq + 1) + (xcd - xr) * xq) + (blockIdx.x >> 3);
    const int node0 = b * npb;
    const int nn = min(npb, N - node0);
    const int c16 = lane & 15, Q = lane >> 4;

    const int seg = tid >> 1, par = tid & 1;
    const u32 v = (seg < nChunks) ? offT[b * nChunks + seg] : 0u;
    if (tid < MAX_NPB) ncnt[tid] = 0;
    const u32 slen = v >> 16;
    const u32 sidx = (u32)seg * (u32)chunk + (v & 0xffffu);
    const u32 inc0 = wave_incl_scan(par ? 0u : slen);
    if (lane == 63) wsum[wv] = inc0;
    __syncthreads();
    u32 spre = inc0 - slen, M = 0;
#pragma unroll
    for (int k = 0; k < B_THREADS / 64; ++k) { const u32 t = wsum[k]; if (k < wv) spre += t; M += t; }

    f32x4 tac[4];
#pragma unroll
    for (int m = 0; m < 4; ++m) tac[m] = (f32x4){0.f, 0.f, 0.f, 0.f};
    const char* xc = (const char*)x + (lane & 7) * 16;
    const u32 myrow2 = (u32)(wv * 16 + c16) * 0x10001u;

    for (u32 t0 = 0; t0 < M; t0 += B_CAP) {
        const u32 Mt = min((u32)B_CAP, M - t0);
        if (t0 > 0) { __syncthreads(); if (tid < MAX_NPB) ncnt[tid] = 0; }
        {
            u32 i = par;
            if (t0 > spre) i = (t0 - spre + 1 - par) / 2 * 2 + par;
#pragma clang loop unroll(disable) vectorize(disable) interleave(disable)
            for (; i < slen; i += 2) {
                const u32 pos = spre + i - t0;
                if (pos >= B_CAP) break;
                sIdx[pos] = sidx + i;
            }
        }
        __syncthreads();
        u32 csrc[B_RPT], chw[B_RPT];
        {
            u64 r[B_RPT];
#pragma unroll
            for (int k = 0; k < B_RPT; ++k) {
                const u32 gi = sIdx[min((u32)(k * B_THREADS + tid), Mt - 1)];
                r[k] = *(const u64*)((const char*)recs + (size_t)gi * 8);
            }
#pragma unroll
            for (int k = 0; k < B_RPT; ++k) {
                csrc[k] = (u32)r[k];
                chw[k] = ((u32)(k * B_THREADS + tid) < Mt) ? (u32)(r[k] >> 32) : ~0u;
            }
        }
        u32 rank[B_RPT];
#pragma unroll
        for (int k = 0; k < B_RPT; ++k) {
            if (chw[k] != ~0u) rank[k] = atomicAdd(&ncnt[chw[k] >> 16], 1u);
        }
        __syncthreads();
        u32 tbeg, tcnt;
        {
            const u32 a0 = ncnt[2 * lane], a1 = ncnt[2 * lane + 1];
            const u32 inc = wave_incl_scan(a0 + a1);
            const u32 tileEnd = __shfl(inc, lane | 7, 64);
            const u32 prevEnd = __shfl(inc, (lane & ~7) - 1, 64);
            const u32 tileBegU = (lane < 8) ? 0u : prevEnd;
            const u32 tot = tileEnd - tileBegU;
            const u32 padAll = (tot + 31u) & ~31u;
            const u32 pinc = wave_incl_scan(((lane & 7) == 0) ? padAll : 0u);
            const u32 tbase = pinc - padAll;
            const u32 ex = tbase + (inc - a0 - a1 - tileBegU);
            nbase[2 * lane] = ex; nbase[2 * lane + 1] = ex + a0;
            tbeg = __builtin_amdgcn_readlane(tbase, wv * 8);
            tcnt = __builtin_amdgcn_readlane(tot, wv * 8);
        }
#pragma unroll
        for (int k = 0; k < B_RPT; ++k) {
            if (chw[k] != ~0u) {
                const u32 p = nbase[chw[k] >> 16] + rank[k];
                sSrc[p] = csrc[k]; sNode[p] = (unsigned short)(chw[k] >> 16); sEw[p] = (unsigned short)chw[k];
            }
        }
        if (lane < 32 && (tcnt & 31u) != 0 && lane >= (int)(tcnt & 31u)) {
            const u32 p = tbeg + (tcnt & ~31u) + lane;
            sSrc[p] = 0; sNode[p] = 0xFFFF; sEw[p] = 0;
        }
        __syncthreads();
        const int nsteps = (int)((tcnt + 31u) >> 5);
        if (nsteps > 0) {
            uint4 vA[4];
            auto issue = [&](uint4 (&v)[4], u32 kstep) {
                const uint4 s0 = *(const uint4*)(sSrc + tbeg + kstep * 32 + 4 * (lane >> 3));
                v[0] = *(const uint4*)(xc + (size_t)s0.x * 128); v[1] = *(const uint4*)(xc + (size_t)s0.y * 128);
                v[2] = *(const uint4*)(xc + (size_t)s0.z * 128); v[3] = *(const uint4*)(xc + (size_t)s0.w * 128);
            };
            auto compute = [&](const uint4 (&v)[4], u32 kstep) {
                const u32 kb = tbeg + kstep * 32 + 8 * Q;
                const uint4 nd = *(const uint4*)(sNode + kb), ww = *(const uint4*)(sEw + kb);
                uint4 sf;
                sf.x = ww.x & pk_zero_mask(pk_sub_u16(nd.x, myrow2));
                sf.y = ww.y & pk_zero_mask(pk_sub_u16(nd.y, myrow2));
                sf.z = ww.z & pk_zero_mask(pk_sub_u16(nd.z, myrow2));
                sf.w = ww.w & pk_zero_mask(pk_sub_u16(nd.w, myrow2));
                const bf16x8 sfr = __builtin_bit_cast(bf16x8, sf);
                u32 Ax[4], Ay[4], Bx[4], By[4];
#pragma unroll
                for (int i = 0; i < 4; ++i) {
                    Ax[i] = (u32)__builtin_amdgcn_update_dpp((int)v[i].x, (int)v[i].z, 0x128, 0xf, 0xc, false);
                    Ay[i] = (u32)__builtin_amdgcn_update_dpp((int)v[i].y, (int)v[i].w, 0x128, 0xf, 0xc, false);
                    Bx[i] = (u32)__builtin_amdgcn_update_dpp((int)v[i].z, (int)v[i].x, 0x128, 0xf, 0x3, false);
                    By[i] = (u32)__builtin_amdgcn_update_dpp((int)v[i].w, (int)v[i].y, 0x128, 0xf, 0x3, false);
                }
                uint4 f0, f1, f2, f3;
                f0.x = __builtin_amdgcn_perm(Ax[1], Ax[0], 0x05040100); f1.x = __builtin_amdgcn_perm(Ax[1], Ax[0], 0x07060302);
                f2.x = __builtin_amdgcn_perm(Ay[1], Ay[0], 0x05040100); f3.x = __builtin_amdgcn_perm(Ay[1], Ay[0], 0x07060302);
                f0.y = __builtin_amdgcn_perm(Ax[3], Ax[2], 0x05040100); f1.y = __builtin_amdgcn_perm(Ax[3], Ax[2], 0x07060302);
                f2.y = __builtin_amdgcn_perm(Ay[3], Ay[2], 0x05040100); f3.y = __builtin_amdgcn_perm(Ay[3], Ay[2], 0x07060302);
                f0.z = __builtin_amdgcn_perm(Bx[1], Bx[0], 0x05040100); f1.z = __builtin_amdgcn_perm(Bx[1], Bx[0], 0x07060302);
                f2.z = __builtin_amdgcn_perm(By[1], By[0], 0x05040100); f3.z = __builtin_amdgcn_perm(By[1], By[0], 0x07060302);
                f0.w = __builtin_amdgcn_perm(Bx[3], Bx[2], 0x05040100); f1.w = __builtin_amdgcn_perm(Bx[3], Bx[2], 0x07060302);
                f2.w = __builtin_amdgcn_perm(By[3], By[2], 0x05040100); f3.w = __builtin_amdgcn_perm(By[3], By[2], 0x07060302);
                tac[0] = __builtin_amdgcn_mfma_f32_16x16x32_bf16(__builtin_bit_cast(bf16x8, f0), sfr, tac[0], 0, 0, 0);
                tac[1] = __builtin_amdgcn_mfma_f32_16x16x32_bf16(__builtin_bit_cast(bf16x8, f1), sfr, tac[1], 0, 0, 0);
                tac[2] = __builtin_amdgcn_mfma_f32_16x16x32_bf16(__builtin_bit_cast(bf16x8, f2), sfr, tac[2], 0, 0, 0);
                tac[3] = __builtin_amdgcn_mfma_f32_16x16x32_bf16(__builtin_bit_cast(bf16x8, f3), sfr, tac[3], 0, 0, 0);
            };
            issue(vA, 0);
            for (int st = 0; st + 1 < nsteps; ++st) {
                uint4 vB[4];
                issue(vB, st + 1);
                compute(vA, st);
#pragma unroll
                for (int k = 0; k < 4; ++k) {
                    asm volatile("" : "+v"(vB[k].x), "+v"(vB[k].y), "+v"(vB[k].z), "+v"(vB[k].w));
                    vA[k] = vB[k];
                }
            }
            compute(vA, nsteps - 1);
        }
    }

    if (wv * 16 < nn) {
        const int nl = wv * 16 + c16;
        const bool ok = nl < nn;
        bf16x8 bfr[4];
        {
            uint4 z = make_uint4(0, 0, 0, 0);
            uint4 x0 = z, x1 = z;
            if (ok) {
                const unsigned short* xr = x + (size_t)(node0 + nl) * D + 8 * Q;
                x0 = *(const uint4*)(xr);
                x1 = *(const uint4*)(xr + 32);
            }
            uint4 t0 = make_uint4(pack_bf16(tac[0][0], tac[1][0]), pack_bf16(tac[2][0], tac[3][0]),
                                  pack_bf16(tac[0][1], tac[1][1]), pack_bf16(tac[2][1], tac[3][1]));
            uint4 t1 = make_uint4(pack_bf16(tac[0][2], tac[1][2]), pack_bf16(tac[2][2], tac[3][2]),
                                  pack_bf16(tac[0][3], tac[1][3]), pack_bf16(tac[2][3], tac[3][3]));
            bfr[0] = __builtin_bit_cast(bf16x8, x0); bfr[1] = __builtin_bit_cast(bf16x8, x1);
            bfr[2] = __builtin_bit_cast(bf16x8, t0); bfr[3] = __builtin_bit_cast(bf16x8, t1);
        }
        u32 o[8];
#pragma unroll
        for (int ct = 0; ct < 4; ++ct) {
            f32x4 c4 = {0.f, 0.f, 0.f, 0.f};
#pragma unroll
            for (int s = 0; s < 4; ++s) {
                bf16x8 af = __builtin_bit_cast(bf16x8, wfrag[(s * 4 + ct) * 64 + lane]);
                c4 = __builtin_amdgcn_mfma_f32_16x16x32_bf16(af, bfr[s], c4, 0, 0, 0);
            }
            o[2 * ct] = pack_bf16(c4[0], c4[1]);
            o[2 * ct + 1] = pack_bf16(c4[2], c4[3]);
        }
        if (ok) {
            uint4* op = (uint4*)(out + (size_t)(node0 + nl) * D + 16 * Q);
            op[0] = make_uint4(o[0], o[1], o[2], o[3]);
            op[1] = make_uint4(o[4], o[5], o[6], o[7]);
        }
    }
}

extern "C" void kernel_launch(void* const* d_in, const int* in_sizes, int n_in,
                              void* d_out, int out_size, void* d_ws, size_t ws_size,
                              hipStream_t stream) {
    const unsigned short* x = (const unsigned short*)d_in[0];
    const int* src = (const int*)d_in[1];
    const int* dst = (const int*)d_in[2];
    const unsigned short* ew = (const unsigned short*)d_in[3];
    const unsigned short* w = (const unsigned short*)d_in[4];
    unsigned short* out = (unsigned short*)d_out;
    const int N = in_sizes[0] / D;
    const int E = in_sizes[1];

    int npb = (N + MAX_BUCKETS - 1) / MAX_BUCKETS;
    if (npb < 1) npb = 1;
    const int nBuckets = (N + npb - 1) / npb;
    const u32 magic = (u32)((0x100000000ull / (u64)npb) + 1ull);

    int chunk = (E + MAX_CHUNKS - 1) / MAX_CHUNKS;
    chunk = (chunk + 3) & ~3;
    if (chunk < 4) chunk = 4;
    int nChunks = (E + chunk - 1) / chunk;
    if (nChunks < 1) nChunks = 1;

    char* ws = (char*)d_ws;
    unsigned short* wfrag = (unsigned short*)ws;
    u32* offT = (u32*)(ws + 16384);
    u64* recs = (u64*)(ws + 16384 + (size_t)MAX_BUCKETS * MAX_CHUNKS * 4);

    bin_kernel<<<nChunks, A_THREADS, 0, stream>>>(src, dst, ew, w, E, chunk, npb, magic, nBuckets, nChunks,
                                                  recs, offT, wfrag);
    gather_kernel<<<nBuckets, B_THREADS, 0, stream>>>(x, recs, offT, (const uint4*)wfrag, out,
                                                      N, npb, nChunks, chunk);
}
```

```cpp
#include <hip/hip_runtime.h>
#include <hip/hip_bf16.h>
#include <stdint.h>


typedef __attribute__((ext_vector_type(8))) short bf16x8;
typedef __attribute__((ext_vector_type(4))) float f32x4;
typedef unsigned long long u64;
typedef unsigned int u32;

#define D 64
#define A_THREADS 1024
#define A_EPT 4
#define A_CHUNK_MAX (A_THREADS * A_EPT)
#define MAX_BUCKETS 512
#define MAX_CHUNKS 256
#define B_THREADS 512
#define MAX_NPB 128
#define B_TILES (MAX_NPB / 16)
#define B_RPT 4
#define B_CAP (B_THREADS * B_RPT)
#define B_SCAP (B_CAP + 32 * B_TILES)
#define NPH 4
#define NCNT (MAX_NPB * NPH)
#define CPL (NCNT / 64)
#define ACC_STRIDE 68

__device__ __forceinline__ u32 pack_bf16(float a, float b) {
    __hip_bfloat16 ha = __float2bfloat16(a), hb = __float2bfloat16(b);
    return (u32)__builtin_bit_cast(unsigned short, ha) | ((u32)__builtin_bit_cast(unsigned short, hb) << 16);
}

__device__ __forceinline__ u32 wave_incl_scan(u32 v) {
    v += (u32)__builtin_amdgcn_update_dpp(0, (int)v, 0x111, 0xf, 0xf, false);
    v += (u32)__builtin_amdgcn_update_dpp(0, (int)v, 0x112, 0xf, 0xf, false);
    v += (u32)__builtin_amdgcn_update_dpp(0, (int)v, 0x114, 0xf, 0xf, false);
    v += (u32)__builtin_amdgcn_update_dpp(0, (int)v, 0x118, 0xf, 0xf, false);
    v += (u32)__builtin_amdgcn_update_dpp(0, (int)v, 0x142, 0xa, 0xf, false);
    v += (u32)__builtin_amdgcn_update_dpp(0, (int)v, 0x143, 0xc, 0xf, false);
    return v;
}

__device__ __forceinline__ u32 pk_sub_u16(u32 a, u32 b) {
    typedef __attribute__((ext_vector_type(2))) unsigned short us2;
    us2 r = __builtin_bit_cast(us2, a) - __builtin_bit_cast(us2, b);
    return __builtin_bit_cast(u32, r);
}
__device__ __forceinline__ u32 pk_zero_mask(u32 d) {
    typedef __attribute__((ext_vector_type(2))) unsigned short us2;
    us2 dv = __builtin_bit_cast(us2, d);
    us2 one = {1, 1};
    us2 m = __builtin_elementwise_min(dv, one) - one;
    return __builtin_bit_cast(u32, m);
}

__global__ __launch_bounds__(A_THREADS) void bin_kernel(
    const int* __restrict__ src, const int* __restrict__ dst, const unsigned short* __restrict__ ew,
    const unsigned short* __restrict__ w, int E, int chunk, int npb, u32 magic, int nBuckets, int nChunks, int phstep,
    u64* __restrict__ recs, u32* __restrict__ offT, unsigned short* __restrict__ wfrag)
{
    __shared__ __attribute__((aligned(16))) u32 hist[MAX_BUCKETS];
    __shared__ __attribute__((aligned(16))) u32 hbase[MAX_BUCKETS];
    __shared__ __attribute__((aligned(16))) u32 wsum[MAX_BUCKETS / 64];
    __shared__ __attribute__((aligned(16))) u64 stage[A_CHUNK_MAX];

    const int tid = threadIdx.x, lane = tid & 63, wv = tid >> 6;
    const int c = blockIdx.x;
    const int e0 = c * chunk;
    const int cnt = min(chunk, E - e0);

    int d[A_EPT], s[A_EPT]; u32 wgt[A_EPT];
    const int i0 = tid * A_EPT;
    if (i0 + A_EPT <= cnt) {
        int4 dv = *(const int4*)(dst + e0 + i0);
        int4 sv = *(const int4*)(src + e0 + i0);
        uint2 wv2 = *(const uint2*)(ew + e0 + i0);
        d[0] = dv.x; d[1] = dv.y; d[2] = dv.z; d[3] = dv.w;
        s[0] = sv.x; s[1] = sv.y; s[2] = sv.z; s[3] = sv.w;
        wgt[0] = wv2.x & 0xffffu; wgt[1] = wv2.x >> 16; wgt[2] = wv2.y & 0xffffu; wgt[3] = wv2.y >> 16;
    } else {
#pragma unroll
        for (int k = 0; k < A_EPT; ++k) {
            if (i0 + k < cnt) { d[k] = dst[e0 + i0 + k]; s[k] = src[e0 + i0 + k]; wgt[k] = ew[e0 + i0 + k]; }
            else { d[k] = -1; s[k] = 0; wgt[k] = 0; }
        }
    }
    if (tid < MAX_BUCKETS) hist[tid] = 0;

    if (c == 0) {
        const int l = tid & 63, sc = tid >> 6, ks = sc >> 2, ct = sc & 3;
        const int r = l & 15, q = l >> 4;
        const int f = 16 * (r >> 2) + 4 * ct + (r & 3);
        unsigned short v[8];
#pragma unroll
        for (int j = 0; j < 8; ++j) {
            const int cc = 4 * q + 2 * (ks - 2) + (j >> 2);
            const int krow = (ks < 2) ? (32 * ks + 8 * q + j) : (64 + 8 * (cc & 7) + 4 * (cc >> 3) + (j & 3));
            v[j] = w[krow * D + f];
        }
        uint4 o;
        o.x = v[0] | ((u32)v[1] << 16); o.y = v[2] | ((u32)v[3] << 16);
        o.z = v[4] | ((u32)v[5] << 16); o.w = v[6] | ((u32)v[7] << 16);
        ((uint4*)wfrag)[tid] = o;
    }
    __syncthreads();
    int bk[A_EPT]; u32 rank[A_EPT]; u32 hi[A_EPT];
#pragma unroll
    for (int k = 0; k < A_EPT; ++k) {
        bk[k] = -1; hi[k] = 0; rank[k] = 0;
        if (d[k] >= 0) {
            u32 q = __umulhi((u32)d[k], magic);
            int r = d[k] - (int)q * npb;
            if (r < 0) { q -= 1; r += npb; }
            if (r >= npb) { q += 1; r -= npb; }
            bk[k] = (int)q;
            const u32 ph = (u32)(s[k] >= phstep) + (u32)(s[k] >= 2 * phstep) + (u32)(s[k] >= 3 * phstep);
            hi[k] = ((((u32)r >> 4) * (16 * NPH) + (NPH == 4 ? ph : NPH == 2 ? (ph >> 1) : 0u) * 16 + ((u32)r & 15)) << 16) | wgt[k];
            rank[k] = atomicAdd(&hist[q], 1u);
        }
    }
    __syncthreads();
    u32 h = 0, inc = 0;
    if (tid < MAX_BUCKETS) {
        h = hist[tid];
        inc = wave_incl_scan(h);
        if (lane == 63) wsum[wv] = inc;
    }
    __syncthreads();
    if (tid < MAX_BUCKETS) {
        u32 wb = 0;
#pragma unroll
        for (int k = 0; k < MAX_BUCKETS / 64; ++k) { const u32 t = wsum[k]; if (k < wv) wb += t; }
        const u32 base = inc - h + wb;
        hbase[tid] = base;
        if (tid < nBuckets) offT[tid * nChunks + c] = base | (h << 16);
    }
    __syncthreads();
#pragma unroll
    for (int k = 0; k < A_EPT; ++k) {
        if (bk[k] >= 0) stage[hbase[bk[k]] + rank[k]] = ((u64)hi[k] << 32) | (u32)s[k];
    }
    __syncthreads();
    u64* outp = recs + (size_t)c * chunk;
    for (int i = tid * 2; i < cnt; i += A_THREADS * 2) {
        if (i + 1 < cnt) *(uint4*)(outp + i) = *(const uint4*)(stage + i);
        else outp[i] = stage[i];
    }
}

__global__ __launch_bounds__(B_THREADS) void gather_kernel(
    const unsigned short* __restrict__ x, const u64* __restrict__ recs, const u32* __restrict__ offT,
    const uint4* __restrict__ wfrag, unsigned short* __restrict__ out,
    int N, int npb, int nChunks, int chunk)
{
    __shared__ __attribute__((aligned(16))) u32 sSrc[B_SCAP];
    __shared__ __attribute__((aligned(16))) unsigned short sNode[B_SCAP];
    __shared__ __attribute__((aligned(16))) unsigned short sEw[B_SCAP];
    __shared__ __attribute__((aligned(16))) uint4 sW[16 * 64];
    __shared__ __attribute__((aligned(16))) u32 ncnt[NCNT];
    __shared__ __attribute__((aligned(16))) u32 nbase[NCNT];
    __shared__ u32 sMore[2];

    const int tid = threadIdx.x, lane = tid & 63, wv = __builtin_amdgcn_readfirstlane(tid >> 6);
    const int nB = gridDim.x, xq = nB >> 3, xr = nB & 7, xcd = blockIdx.x & 7;
    const int b = (xcd < xr ? xcd * (xq + 1) : xr * (xq + 1) + (xcd - xr) * xq) + (blockIdx.x >> 3);
    const int node0 = b * npb;
    const int nn = min(npb, N - node0);
    const int c16 = lane & 15, Q = lane >> 4;

    const int seg = wv * 32 + lane;
    const u32 v = (lane < 32 && seg < nChunks) ? offT[b * nChunks + seg] : 0u;
    const uint4 wf0 = wfrag[tid], wf1 = wfrag[B_THREADS + tid];
    if (tid < NCNT) ncnt[tid] = 0;
    if (tid < 2) sMore[tid] = 0;
    __syncthreads();
    const u32 slen = v >> 16;
    const u32 sinc = wave_incl_scan(slen);
    const u32 spre = sinc - slen;
    const u32 sgb = (u32)seg * (u32)chunk + (v & 0xffffu) - spre;
    const u32 Mw = __builtin_amdgcn_readlane(sinc, 63);

    f32x4 tac[4];
#pragma unroll
    for (int m = 0; m < 4; ++m) tac[m] = (f32x4){0.f, 0.f, 0.f, 0.f};
    const char* xc = (const char*)x + (lane & 7) * 16;
    const u32 myrow2 = (u32)c16 * 0x10001u;

    for (u32 pass = 0;; ++pass) {
        const u32 t0 = pass * (64 * B_RPT);
        u32 csrc[B_RPT], chw[B_RPT];
        {
            u64 r[B_RPT];
#pragma unroll
            for (int k = 0; k < B_RPT; ++k) {
                const u32 i = min(t0 + k * 64 + lane, Mw > 0 ? Mw - 1 : 0u);
                int lo = 0;
#pragma unroll
                for (int step = 16; step >= 1; step >>= 1) {
                    const u32 pv = (u32)__builtin_amdgcn_ds_bpermute((lo + step) << 2, (int)spre);
                    lo = (pv <= i) ? lo + step : lo;
                }
                const u32 gi = (u32)__builtin_amdgcn_ds_bpermute(lo << 2, (int)sgb) + i;
                r[k] = *(const u64*)((const char*)recs + (size_t)gi * 8);
            }
#pragma unroll
            for (int k = 0; k < B_RPT; ++k) {
                csrc[k] = (u32)r[k];
                chw[k] = (t0 + k * 64 + lane < Mw) ? (u32)(r[k] >> 32) : ~0u;
            }
        }
        if (pass == 0) { sW[tid] = wf0; sW[B_THREADS + tid] = wf1; }
        if (lane == 0 && Mw > t0 + 64 * B_RPT) sMore[pass & 1] = 1;
        u32 rank[B_RPT];
#pragma unroll
        for (int k = 0; k < B_RPT; ++k) {
            if (chw[k] != ~0u) rank[k] = atomicAdd(&ncnt[chw[k] >> 16], 1u);
        }
        __syncthreads();
        if (tid == 0) sMore[(pass + 1) & 1] = 0;
        u32 tbeg, tcnt;
        {
            u32 a[CPL], sum = 0;
#pragma unroll
            for (int k = 0; k < CPL; ++k) { a[k] = ncnt[CPL * lane + k]; sum += a[k]; }
            const u32 inc = wave_incl_scan(sum);
            const u32 tileEnd = __shfl(inc, lane | 7, 64);
            const u32 prevEnd = __shfl(inc, (lane & ~7) - 1, 64);
            const u32 tileBegU = (lane < 8) ? 0u : prevEnd;
            const u32 tot = tileEnd - tileBegU;
            const u32 padAll = (tot + 31u) & ~31u;
            const u32 pinc = wave_incl_scan(((lane & 7) == 0) ? padAll : 0u);
            const u32 tbase = pinc - padAll;
            u32 ex = tbase + (inc - sum - tileBegU);
#pragma unroll
            for (int k = 0; k < CPL; ++k) { nbase[CPL * lane + k] = ex; ex += a[k]; }
            tbeg = __builtin_amdgcn_readlane(tbase, wv * 8);
            tcnt = __builtin_amdgcn_readlane(tot, wv * 8);
        }
#pragma unroll
        for (int k = 0; k < B_RPT; ++k) {
            if (chw[k] != ~0u) {
                const u32 p = nbase[chw[k] >> 16] + rank[k];
                sSrc[p] = csrc[k]; sNode[p] = (unsigned short)((chw[k] >> 16) & 15u); sEw[p] = (unsigned short)chw[k];
            }
        }
        if (lane < 32 && (tcnt & 31u) != 0 && lane >= (int)(tcnt & 31u)) {
            const u32 p = tbeg + (tcnt & ~31u) + lane;
            sSrc[p] = 0; sNode[p] = 0xFFFF; sEw[p] = 0;
        }
        __syncthreads();
        const u32 again = sMore[pass & 1];
        const int nsteps = (int)((tcnt + 31u) >> 5);
        if (nsteps > 0) {
            uint4 vA[4];
            auto issue = [&](uint4 (&v)[4], u32 kstep) {
                const uint4 s0 = *(const uint4*)(sSrc + tbeg + kstep * 32 + 4 * (lane >> 3));
                v[0] = *(const uint4*)(xc + (size_t)s0.x * 128); v[1] = *(const uint4*)(xc + (size_t)s0.y * 128);
                v[2] = *(const uint4*)(xc + (size_t)s0.z * 128); v[3] = *(const uint4*)(xc + (size_t)s0.w * 128);
            };
            auto compute = [&](const uint4 (&v)[4], u32 kstep) {
                const u32 kb = tbeg + kstep * 32 + 8 * Q;
                const uint4 nd = *(const uint4*)(sNode + kb), ww = *(const uint4*)(sEw + kb);
                uint4 sf;
                sf.x = ww.x & pk_zero_mask(pk_sub_u16(nd.x, myrow2));
                sf.y = ww.y & pk_zero_mask(pk_sub_u16(nd.y, myrow2));
                sf.z = ww.z & pk_zero_mask(pk_sub_u16(nd.z, myrow2));
                sf.w = ww.w & pk_zero_mask(pk_sub_u16(nd.w, myrow2));
                const bf16x8 sfr = __builtin_bit_cast(bf16x8, sf);
                u32 Ax[4], Ay[4], Bx[4], By[4];
#pragma unroll
                for (int i = 0; i < 4; ++i) {
                    Ax[i] = (u32)__builtin_amdgcn_update_dpp((int)v[i].x, (int)v[i].z, 0x128, 0xf, 0xc, false);
                    Ay[i] = (u32)__builtin_amdgcn_update_dpp((int)v[i].y, (int)v[i].w, 0x128, 0xf, 0xc, false);
                    Bx[i] = (u32)__builtin_amdgcn_update_dpp((int)v[i].z, (int)v[i].x, 0x128, 0xf, 0x3, false);
                    By[i] = (u32)__builtin_amdgcn_update_dpp((int)v[i].w, (int)v[i].y, 0x128, 0xf, 0x3, false);
                }
                uint4 f0, f1, f2, f3;
                f0.x = __builtin_amdgcn_perm(Ax[1], Ax[0], 0x05040100); f1.x = __builtin_amdgcn_perm(Ax[1], Ax[0], 0x07060302);
                f2.x = __builtin_amdgcn_perm(Ay[1], Ay[0], 0x05040100); f3.x = __builtin_amdgcn_perm(Ay[1], Ay[0], 0x07060302);
                f0.y = __builtin_amdgcn_perm(Ax[3], Ax[2], 0x05040100); f1.y = __builtin_amdgcn_perm(Ax[3], Ax[2], 0x07060302);
                f2.y = __builtin_amdgcn_perm(Ay[3], Ay[2], 0x05040100); f3.y = __builtin_amdgcn_perm(Ay[3], Ay[2], 0x07060302);
                f0.z = __builtin_amdgcn_perm(Bx[1], Bx[0], 0x05040100); f1.z = __builtin_amdgcn_perm(Bx[1], Bx[0], 0x07060302);
                f2.z = __builtin_amdgcn_perm(By[1], By[0], 0x05040100); f3.z = __builtin_amdgcn_perm(By[1], By[0], 0x07060302);
                f0.w = __builtin_amdgcn_perm(Bx[3], Bx[2], 0x05040100); f1.w = __builtin_amdgcn_perm(Bx[3], Bx[2], 0x07060302);
                f2.w = __builtin_amdgcn_perm(By[3], By[2], 0x05040100); f3.w = __builtin_amdgcn_perm(By[3], By[2], 0x07060302);
                tac[0] = __builtin_amdgcn_mfma_f32_16x16x32_bf16(__builtin_bit_cast(bf16x8, f0), sfr, tac[0], 0, 0, 0);
                tac[1] = __builtin_amdgcn_mfma_f32_16x16x32_bf16(__builtin_bit_cast(bf16x8, f1), sfr, tac[1], 0, 0, 0);
                tac[2] = __builtin_amdgcn_mfma_f32_16x16x32_bf16(__builtin_bit_cast(bf16x8, f2), sfr, tac[2], 0, 0, 0);
                tac[3] = __builtin_amdgcn_mfma_f32_16x16x32_bf16(__builtin_bit_cast(bf16x8, f3), sfr, tac[3], 0, 0, 0);
            };
            issue(vA, 0);
            for (int st = 0; st + 1 < nsteps; ++st) {
                uint4 vB[4];
                issue(vB, st + 1);
                compute(vA, st);
#pragma unroll
                for (int k = 0; k < 4; ++k) {
                    asm volatile("" : "+v"(vB[k].x), "+v"(vB[k].y), "+v"(vB[k].z), "+v"(vB[k].w));
                    vA[k] = vB[k];
                }
            }
            compute(vA, nsteps - 1);
        }
        if (!again) break;
        __syncthreads();
        if (tid < NCNT) ncnt[tid] = 0;
        __syncthreads();
    }

    if (wv * 16 < nn) {
        const int nl = wv * 16 + c16;
        const bool ok = nl < nn;
        bf16x8 bfr[4];
        {
            uint4 z = make_uint4(0, 0, 0, 0);
            uint4 x0 = z, x1 = z;
            if (ok) {
                const unsigned short* xr = x + (size_t)(node0 + nl) * D + 8 * Q;
                x0 = *(const uint4*)(xr);
                x1 = *(const uint4*)(xr + 32);
            }
            uint4 t0 = make_uint4(pack_bf16(tac[0][0], tac[1][0]), pack_bf16(tac[2][0], tac[3][0]),
                                  pack_bf16(tac[0][1], tac[1][1]), pack_bf16(tac[2][1], tac[3][1]));
            uint4 t1 = make_uint4(pack_bf16(tac[0][2], tac[1][2]), pack_bf16(tac[2][2], tac[3][2]),
                                  pack_bf16(tac[0][3], tac[1][3]), pack_bf16(tac[2][3], tac[3][3]));
            bfr[0] = __builtin_bit_cast(bf16x8, x0); bfr[1] = __builtin_bit_cast(bf16x8, x1);
            bfr[2] = __builtin_bit_cast(bf16x8, t0); bfr[3] = __builtin_bit_cast(bf16x8, t1);
        }
        u32 o[8];
#pragma unroll
        for (int ct = 0; ct < 4; ++ct) {
            f32x4 c4 = {0.f, 0.f, 0.f, 0.f};
#pragma unroll
            for (int s = 0; s < 4; ++s) {
                bf16x8 af = __builtin_bit_cast(bf16x8, sW[(s * 4 + ct) * 64 + lane]);
                c4 = __builtin_amdgcn_mfma_f32_16x16x32_bf16(af, bfr[s], c4, 0, 0, 0);
            }
            o[2 * ct] = pack_bf16(c4[0], c4[1]);
            o[2 * ct + 1] = pack_bf16(c4[2], c4[3]);
        }
        if (ok) {
            uint4* op = (uint4*)(out + (size_t)(node0 + nl) * D + 16 * Q);
            op[0] = make_uint4(o[0], o[1], o[2], o[3]);
            op[1] = make_uint4(o[4], o[5], o[6], o[7]);
        }
    }
}

extern "C" void kernel_launch(void* const* d_in, const int* in_sizes, int n_in,
                              void* d_out, int out_size, void* d_ws, size_t ws_size,
                              hipStream_t stream) {
    const unsigned short* x = (const unsigned short*)d_in[0];
    const int* src = (const int*)d_in[1];
    const int* dst = (const int*)d_in[2];
    const unsigned short* ew = (const unsigned short*)d_in[3];
    const unsigned short* w = (const unsigned short*)d_in[4];
    unsigned short* out = (unsigned short*)d_out;
    const int N = in_sizes[0] / D;
    const int E = in_sizes[1];

    int npb = (N + MAX_BUCKETS - 1) / MAX_BUCKETS;
    if (npb < 1) npb = 1;
    const int nBuckets = (N + npb - 1) / npb;
    const u32 magic = (u32)((0x100000000ull / (u64)npb) + 1ull);

    int chunk = (E + MAX_CHUNKS - 1) / MAX_CHUNKS;
    chunk = (chunk + 3) & ~3;
    if (chunk < 4) chunk = 4;
    int nChunks = (E + chunk - 1) / chunk;
    if (nChunks < 1) nChunks = 1;

    char* ws = (char*)d_ws;
    unsigned short* wfrag = (unsigned short*)ws;
    u32* offT = (u32*)(ws + 16384);
    u64* recs = (u64*)(ws + 16384 + (size_t)MAX_BUCKETS * MAX_CHUNKS * 4);

    bin_kernel<<<nChunks, A_THREADS, 0, stream>>>(src, dst, ew, w, E, chunk, npb, magic, nBuckets, nChunks,
                                                  (N + 3) / 4, recs, offT, wfrag);
    gather_kernel<<<nBuckets, B_THREADS, 0, stream>>>(x, recs, offT, (const uint4*)wfrag, out,
                                                      N, npb, nChunks, chunk);
}
```

```cpp
#include <hip/hip_runtime.h>
#include <hip/hip_bf16.h>
#include <stdint.h>


typedef __attribute__((ext_vector_type(8))) short bf16x8;
typedef __attribute__((ext_vector_type(4))) float f32x4;
typedef unsigned long long u64;
typedef unsigned int u32;

#define D 64
#define A_THREADS 1024
#define A_EPT 4
#define A_CHUNK_MAX (A_THREADS * A_EPT)
#define MAX_BUCKETS 512
#define MAX_CHUNKS 256
#define B_THREADS 512
#define MAX_NPB 128
#define B_TILES (MAX_NPB / 16)
#define B_RPT 4
#define B_CAP (B_THREADS * B_RPT)
#define B_SCAP (B_CAP + 32 * B_TILES)
#define NPH 4
#define NCNT (MAX_NPB * NPH)
#define CPL (NCNT / 64)

__device__ __forceinline__ u32 pack_bf16(float a, float b) {
    __hip_bfloat16 ha = __float2bfloat16(a), hb = __float2bfloat16(b);
    return (u32)__builtin_bit_cast(unsigned short, ha) | ((u32)__builtin_bit_cast(unsigned short, hb) << 16);
}

__device__ __forceinline__ u32 wave_incl_scan(u32 v) {
    v += (u32)__builtin_amdgcn_update_dpp(0, (int)v, 0x111, 0xf, 0xf, false);
    v += (u32)__builtin_amdgcn_update_dpp(0, (int)v, 0x112, 0xf, 0xf, false);
    v += (u32)__builtin_amdgcn_update_dpp(0, (int)v, 0x114, 0xf, 0xf, false);
    v += (u32)__builtin_amdgcn_update_dpp(0, (int)v, 0x118, 0xf, 0xf, false);
    v += (u32)__builtin_amdgcn_update_dpp(0, (int)v, 0x142, 0xa, 0xf, false);
    v += (u32)__builtin_amdgcn_update_dpp(0, (int)v, 0x143, 0xc, 0xf, false);
    return v;
}

__device__ __forceinline__ u32 pk_sub_u16(u32 a, u32 b) {
    typedef __attribute__((ext_vector_type(2))) unsigned short us2;
    us2 r = __builtin_bit_cast(us2, a) - __builtin_bit_cast(us2, b);
    return __builtin_bit_cast(u32, r);
}
__device__ __forceinline__ u32 pk_zero_mask(u32 d) {
    typedef __attribute__((ext_vector_type(2))) unsigned short us2;
    us2 dv = __builtin_bit_cast(us2, d);
    us2 one = {1, 1};
    us2 m = __builtin_elementwise_min(dv, one) - one;
    return __builtin_bit_cast(u32, m);
}

__global__ __launch_bounds__(A_THREADS) void bin_kernel(
    const int* __restrict__ src, const int* __restrict__ dst, const unsigned short* __restrict__ ew,
    const unsigned short* __restrict__ w, int E, int chunk, int npb, u32 magic, int nBuckets, int nChunks, int phstep,
    u64* __restrict__ recs, u32* __restrict__ offT, unsigned short* __restrict__ wfrag)
{
    __shared__ __attribute__((aligned(16))) u32 hist[MAX_BUCKETS];
    __shared__ __attribute__((aligned(16))) u32 hbase[MAX_BUCKETS];
    __shared__ __attribute__((aligned(16))) u32 wsum[MAX_BUCKETS / 64];
    __shared__ __attribute__((aligned(16))) u64 stage[A_CHUNK_MAX];

    const int tid = threadIdx.x, lane = tid & 63, wv = tid >> 6;
    const int c = blockIdx.x;
    if (c == nChunks) {
        const int l = tid & 63, sc = tid >> 6, ks = sc >> 2, ct = sc & 3;
        const int r = l & 15, q = l >> 4;
        const int f = 16 * (r >> 2) + 4 * ct + (r & 3);
        unsigned short v[8];
#pragma unroll
        for (int j = 0; j < 8; ++j) {
            const int cc = 4 * q + 2 * (ks - 2) + (j >> 2);
            const int krow = (ks < 2) ? (32 * ks + 8 * q + j) : (64 + 8 * (cc & 7) + 4 * (cc >> 3) + (j & 3));
            v[j] = w[krow * D + f];
        }
        uint4 o;
        o.x = v[0] | ((u32)v[1] << 16); o.y = v[2] | ((u32)v[3] << 16);
        o.z = v[4] | ((u32)v[5] << 16); o.w = v[6] | ((u32)v[7] << 16);
        ((uint4*)wfrag)[tid] = o;
        return;
    }
    const int e0 = c * chunk;
    const int cnt = min(chunk, E - e0);

    int d[A_EPT], s[A_EPT]; u32 wgt[A_EPT];
    const int i0 = tid * A_EPT;
    if (i0 + A_EPT <= cnt) {
        int4 dv = *(const int4*)(dst + e0 + i0);
        int4 sv = *(const int4*)(src + e0 + i0);
        uint2 wv2 = *(const uint2*)(ew + e0 + i0);
        d[0] = dv.x; d[1] = dv.y; d[2] = dv.z; d[3] = dv.w;
        s[0] = sv.x; s[1] = sv.y; s[2] = sv.z; s[3] = sv.w;
        wgt[0] = wv2.x & 0xffffu; wgt[1] = wv2.x >> 16; wgt[2] = wv2.y & 0xffffu; wgt[3] = wv2.y >> 16;
    } else {
#pragma unroll
        for (int k = 0; k < A_EPT; ++k) {
            if (i0 + k < cnt) { d[k] = dst[e0 + i0 + k]; s[k] = src[e0 + i0 + k]; wgt[k] = ew[e0 + i0 + k]; }
            else { d[k] = -1; s[k] = 0; wgt[k] = 0; }
        }
    }
    if (tid < MAX_BUCKETS) hist[tid] = 0;

    __syncthreads();
    int bk[A_EPT]; u32 rank[A_EPT]; u32 hi[A_EPT];
#pragma unroll
    for (int k = 0; k < A_EPT; ++k) {
        bk[k] = -1; hi[k] = 0; rank[k] = 0;
        if (d[k] >= 0) {
            u32 q = __umulhi((u32)d[k], magic);
            int r = d[k] - (int)q * npb;
            if (r < 0) { q -= 1; r += npb; }
            if (r >= npb) { q += 1; r -= npb; }
            bk[k] = (int)q;
            const u32 ph = (u32)(s[k] >= phstep) + (u32)(s[k] >= 2 * phstep) + (u32)(s[k] >= 3 * phstep);
            hi[k] = ((((u32)r >> 4) * (16 * NPH) + (NPH == 4 ? ph : NPH == 2 ? (ph >> 1) : 0u) * 16 + ((u32)r & 15)) << 16) | wgt[k];
            rank[k] = atomicAdd(&hist[q], 1u);
        }
    }
    __syncthreads();
    u32 h = 0, inc = 0;
    if (tid < MAX_BUCKETS) {
        h = hist[tid];
        inc = wave_incl_scan(h);
        if (lane == 63) wsum[wv] = inc;
    }
    __syncthreads();
    if (tid < MAX_BUCKETS) {
        u32 wb = 0;
#pragma unroll
        for (int k = 0; k < MAX_BUCKETS / 64; ++k) { const u32 t = wsum[k]; if (k < wv) wb += t; }
        const u32 base = inc - h + wb;
        hbase[tid] = base;
        offT[c * MAX_BUCKETS + tid] = base | (h << 16);
    }
    __syncthreads();
#pragma unroll
    for (int k = 0; k < A_EPT; ++k) {
        if (bk[k] >= 0) stage[hbase[bk[k]] + rank[k]] = ((u64)hi[k] << 32) | (u32)s[k];
    }
    __syncthreads();
    u64* outp = recs + (size_t)c * chunk;
    for (int i = tid * 2; i < cnt; i += A_THREADS * 2) {
        if (i + 1 < cnt) *(uint4*)(outp + i) = *(const uint4*)(stage + i);
        else outp[i] = stage[i];
    }
}

__global__ __launch_bounds__(B_THREADS) void gather_kernel(
    const unsigned short* __restrict__ x, const u64* __restrict__ recs, const u32* __restrict__ offT,
    const uint4* __restrict__ wfrag, unsigned short* __restrict__ out,
    int N, int npb, int nChunks, int chunk)
{
    __shared__ __attribute__((aligned(16))) u32 sSrc[B_SCAP];
    __shared__ __attribute__((aligned(16))) unsigned short sNode[B_SCAP];
    __shared__ __attribute__((aligned(16))) unsigned short sEw[B_SCAP];
    __shared__ __attribute__((aligned(16))) uint4 sW[16 * 64];
    __shared__ __attribute__((aligned(16))) u32 ncnt[NCNT];
    __shared__ __attribute__((aligned(16))) u32 nbase[NCNT];
    __shared__ u32 sMore[2];

    const int tid = threadIdx.x, lane = tid & 63, wv = __builtin_amdgcn_readfirstlane(tid >> 6);
    const int nB = gridDim.x, xq = nB >> 3, xr = nB & 7, xcd = blockIdx.x & 7;
    const int b = (xcd < xr ? xcd * (xq + 1) : xr * (xq + 1) + (xcd - xr) * xq) + (blockIdx.x >> 3);
    const int node0 = b * npb;
    const int nn = min(npb, N - node0);
    const int c16 = lane & 15, Q = lane >> 4;

    const int seg = wv * 32 + lane;
    const u32 v = (lane < 32 && seg < nChunks) ? offT[seg * MAX_BUCKETS + b] : 0u;
    const uint4 wf0 = wfrag[tid], wf1 = wfrag[B_THREADS + tid];
    if (tid < NCNT) ncnt[tid] = 0;
    if (tid < 2) sMore[tid] = 0;
    __syncthreads();
    const u32 slen = v >> 16;
    const u32 sinc = wave_incl_scan(slen);
    const u32 spre = sinc - slen;
    const u32 sgb = (u32)seg * (u32)chunk + (v & 0xffffu) - spre;
    const u32 Mw = __builtin_amdgcn_readlane(sinc, 63);

    f32x4 tac[4];
#pragma unroll
    for (int m = 0; m < 4; ++m) tac[m] = (f32x4){0.f, 0.f, 0.f, 0.f};
    const char* xc = (const char*)x + (lane & 7) * 16;
    const u32 myrow2 = (u32)c16 * 0x10001u;

    for (u32 pass = 0;; ++pass) {
        const u32 t0 = pass * (64 * B_RPT);
        u32 csrc[B_RPT], chw[B_RPT];
        {
            u64 r[B_RPT];
#pragma unroll
            for (int k = 0; k < B_RPT; ++k) {
                const u32 i = min(t0 + k * 64 + lane, Mw > 0 ? Mw - 1 : 0u);
                int lo = 0;
#pragma unroll
                for (int step = 16; step >= 1; step >>= 1) {
                    const u32 pv = (u32)__builtin_amdgcn_ds_bpermute((lo + step) << 2, (int)spre);
                    lo = (pv <= i) ? lo + step : lo;
                }
                const u32 gi = (u32)__builtin_amdgcn_ds_bpermute(lo << 2, (int)sgb) + i;
                r[k] = *(const u64*)((const char*)recs + (size_t)gi * 8);
            }
#pragma unroll
            for (int k = 0; k < B_RPT; ++k) {
                csrc[k] = (u32)r[k];
                chw[k] = (t0 + k * 64 + lane < Mw) ? (u32)(r[k] >> 32) : ~0u;
            }
        }
        if (pass == 0) { sW[tid] = wf0; sW[B_THREADS + tid] = wf1; }
        if (lane == 0 && Mw > t0 + 64 * B_RPT) sMore[pass & 1] = 1;
        u32 rank[B_RPT];
#pragma unroll
        for (int k = 0; k < B_RPT; ++k) {
            if (chw[k] != ~0u) rank[k] = atomicAdd(&ncnt[chw[k] >> 16], 1u);
        }
        __syncthreads();
        if (tid == 0) sMore[(pass + 1) & 1] = 0;
        u32 tbeg, tcnt;
        {
            u32 a[CPL], sum = 0;
#pragma unroll
            for (int k = 0; k < CPL; ++k) { a[k] = ncnt[CPL * lane + k]; sum += a[k]; }
            const u32 inc = wave_incl_scan(sum);
            const u32 tileEnd = __shfl(inc, lane | 7, 64);
            const u32 prevEnd = __shfl(inc, (lane & ~7) - 1, 64);
            const u32 tileBegU = (lane < 8) ? 0u : prevEnd;
            const u32 tot = tileEnd - tileBegU;
            const u32 padAll = (tot + 31u) & ~31u;
            const u32 pinc = wave_incl_scan(((lane & 7) == 0) ? padAll : 0u);
            const u32 tbase = pinc - padAll;
            u32 ex = tbase + (inc - sum - tileBegU);
#pragma unroll
            for (int k = 0; k < CPL; ++k) { nbase[CPL * lane + k] = ex; ex += a[k]; }
            tbeg = __builtin_amdgcn_readlane(tbase, wv * 8);
            tcnt = __builtin_amdgcn_readlane(tot, wv * 8);
        }
#pragma unroll
        for (int k = 0; k < B_RPT; ++k) {
            if (chw[k] != ~0u) {
                const u32 p = nbase[chw[k] >> 16] + rank[k];
                sSrc[p] = csrc[k]; sNode[p] = (unsigned short)((chw[k] >> 16) & 15u); sEw[p] = (unsigned short)chw[k];
            }
        }
        if (lane < 32 && (tcnt & 31u) != 0 && lane >= (int)(tcnt & 31u)) {
            const u32 p = tbeg + (tcnt & ~31u) + lane;
            sSrc[p] = 0; sNode[p] = 0xFFFF; sEw[p] = 0;
        }
        __syncthreads();
        const u32 again = sMore[pass & 1];
        const int nsteps = (int)((tcnt + 31u) >> 5);
        if (nsteps > 0) {
            uint4 vA[4];
            auto issue = [&](uint4 (&v)[4], u32 kstep) {
                const uint4 s0 = *(const uint4*)(sSrc + tbeg + kstep * 32 + 4 * (lane >> 3));
                v[0] = *(const uint4*)(xc + (size_t)s0.x * 128); v[1] = *(const uint4*)(xc + (size_t)s0.y * 128);
                v[2] = *(const uint4*)(xc + (size_t)s0.z * 128); v[3] = *(const uint4*)(xc + (size_t)s0.w * 128);
            };
            auto compute = [&](const uint4 (&v)[4], u32 kstep) {
                const u32 kb = tbeg + kstep * 32 + 8 * Q;
                const uint4 nd = *(const uint4*)(sNode + kb), ww = *(const uint4*)(sEw + kb);
                uint4 sf;
                sf.x = ww.x & pk_zero_mask(pk_sub_u16(nd.x, myrow2));
                sf.y = ww.y & pk_zero_mask(pk_sub_u16(nd.y, myrow2));
                sf.z = ww.z & pk_zero_mask(pk_sub_u16(nd.z, myrow2));
                sf.w = ww.w & pk_zero_mask(pk_sub_u16(nd.w, myrow2));
                const bf16x8 sfr = __builtin_bit_cast(bf16x8, sf);
                u32 Ax[4], Ay[4], Bx[4], By[4];
#pragma unroll
                for (int i = 0; i < 4; ++i) {
                    Ax[i] = (u32)__builtin_amdgcn_update_dpp((int)v[i].x, (int)v[i].z, 0x128, 0xf, 0xc, false);
                    Ay[i] = (u32)__builtin_amdgcn_update_dpp((int)v[i].y, (int)v[i].w, 0x128, 0xf, 0xc, false);
                    Bx[i] = (u32)__builtin_amdgcn_update_dpp((int)v[i].z, (int)v[i].x, 0x128, 0xf, 0x3, false);
                    By[i] = (u32)__builtin_amdgcn_update_dpp((int)v[i].w, (int)v[i].y, 0x128, 0xf, 0x3, false);
                }
                uint4 f0, f1, f2, f3;
                f0.x = __builtin_amdgcn_perm(Ax[1], Ax[0], 0x05040100); f1.x = __builtin_amdgcn_perm(Ax[1], Ax[0], 0x07060302);
                f2.x = __builtin_amdgcn_perm(Ay[1], Ay[0], 0x05040100); f3.x = __builtin_amdgcn_perm(Ay[1], Ay[0], 0x07060302);
                f0.y = __builtin_amdgcn_perm(Ax[3], Ax[2], 0x05040100); f1.y = __builtin_amdgcn_perm(Ax[3], Ax[2], 0x07060302);
                f2.y = __builtin_amdgcn_perm(Ay[3], Ay[2], 0x05040100); f3.y = __builtin_amdgcn_perm(Ay[3], Ay[2], 0x07060302);
                f0.z = __builtin_amdgcn_perm(Bx[1], Bx[0], 0x05040100); f1.z = __builtin_amdgcn_perm(Bx[1], Bx[0], 0x07060302);
                f2.z = __builtin_amdgcn_perm(By[1], By[0], 0x05040100); f3.z = __builtin_amdgcn_perm(By[1], By[0], 0x07060302);
                f0.w = __builtin_amdgcn_perm(Bx[3], Bx[2], 0x05040100); f1.w = __builtin_amdgcn_perm(Bx[3], Bx[2], 0x07060302);
                f2.w = __builtin_amdgcn_perm(By[3], By[2], 0x05040100); f3.w = __builtin_amdgcn_perm(By[3], By[2], 0x07060302);
                tac[0] = __builtin_amdgcn_mfma_f32_16x16x32_bf16(__builtin_bit_cast(bf16x8, f0), sfr, tac[0], 0, 0, 0);
                tac[1] = __builtin_amdgcn_mfma_f32_16x16x32_bf16(__builtin_bit_cast(bf16x8, f1), sfr, tac[1], 0, 0, 0);
                tac[2] = __builtin_amdgcn_mfma_f32_16x16x32_bf16(__builtin_bit_cast(bf16x8, f2), sfr, tac[2], 0, 0, 0);
                tac[3] = __builtin_amdgcn_mfma_f32_16x16x32_bf16(__builtin_bit_cast(bf16x8, f3), sfr, tac[3], 0, 0, 0);
            };
            issue(vA, 0);
            for (int st = 0; st + 1 < nsteps; ++st) {
                uint4 vB[4];
                issue(vB, st + 1);
                compute(vA, st);
#pragma unroll
                for (int k = 0; k < 4; ++k) {
                    asm volatile("" : "+v"(vB[k].x), "+v"(vB[k].y), "+v"(vB[k].z), "+v"(vB[k].w));
                    vA[k] = vB[k];
                }
            }
            compute(vA, nsteps - 1);
        }
        if (!again) break;
        __syncthreads();
        if (tid < NCNT) ncnt[tid] = 0;
        __syncthreads();
    }

    if (wv * 16 < nn) {
        const int nl = wv * 16 + c16;
        const bool ok = nl < nn;
        bf16x8 bfr[4];
        {
            uint4 z = make_uint4(0, 0, 0, 0);
            uint4 x0 = z, x1 = z;
            if (ok) {
                const unsigned short* xr = x + (size_t)(node0 + nl) * D + 8 * Q;
                x0 = *(const uint4*)(xr);
                x1 = *(const uint4*)(xr + 32);
            }
            uint4 t0 = make_uint4(pack_bf16(tac[0][0], tac[1][0]), pack_bf16(tac[2][0], tac[3][0]),
                                  pack_bf16(tac[0][1], tac[1][1]), pack_bf16(tac[2][1], tac[3][1]));
            uint4 t1 = make_uint4(pack_bf16(tac[0][2], tac[1][2]), pack_bf16(tac[2][2], tac[3][2]),
                                  pack_bf16(tac[0][3], tac[1][3]), pack_bf16(tac[2][3], tac[3][3]));
            bfr[0] = __builtin_bit_cast(bf16x8, x0); bfr[1] = __builtin_bit_cast(bf16x8, x1);
            bfr[2] = __builtin_bit_cast(bf16x8, t0); bfr[3] = __builtin_bit_cast(bf16x8, t1);
        }
        u32 o[8];
#pragma unroll
        for (int ct = 0; ct < 4; ++ct) {
            f32x4 c4 = {0.f, 0.f, 0.f, 0.f};
#pragma unroll
            for (int s = 0; s < 4; ++s) {
                bf16x8 af = __builtin_bit_cast(bf16x8, sW[(s * 4 + ct) * 64 + lane]);
                c4 = __builtin_amdgcn_mfma_f32_16x16x32_bf16(af, bfr[s], c4, 0, 0, 0);
            }
            o[2 * ct] = pack_bf16(c4[0], c4[1]);
            o[2 * ct + 1] = pack_bf16(c4[2], c4[3]);
        }
        if (ok) {
            uint4* op = (uint4*)(out + (size_t)(node0 + nl) * D + 16 * Q);
            op[0] = make_uint4(o[0], o[1], o[2], o[3]);
            op[1] = make_uint4(o[4], o[5], o[6], o[7]);
        }
    }
}

__global__ void fb_zero(float* t1, size_t n) {
    for (size_t i = blockIdx.x * (size_t)blockDim.x + threadIdx.x; i < n; i += (size_t)gridDim.x * blockDim.x) t1[i] = 0.f;
}
__global__ void fb_scatter(const unsigned short* __restrict__ x, const int* __restrict__ src, const int* __restrict__ dst,
                           const unsigned short* __restrict__ ew, float* t1, size_t E) {
    for (size_t i = blockIdx.x * (size_t)blockDim.x + threadIdx.x; i < E * D; i += (size_t)gridDim.x * blockDim.x) {
        const size_t e = i / D; const int d = (int)(i % D);
        const float wv = __uint_as_float((u32)ew[e] << 16);
        const float xv = __uint_as_float((u32)x[(size_t)src[e] * D + d] << 16);
        atomicAdd(t1 + (size_t)dst[e] * D + d, wv * xv);
    }
}
__global__ void fb_gemm(const unsigned short* __restrict__ x, const float* __restrict__ t1, const unsigned short* __restrict__ w,
                        unsigned short* __restrict__ out, size_t N) {
    for (size_t i = blockIdx.x * (size_t)blockDim.x + threadIdx.x; i < N * D; i += (size_t)gridDim.x * blockDim.x) {
        const size_t n = i / D; const int e = (int)(i % D);
        float acc = 0.f;
        for (int d = 0; d < D; ++d) {
            const float tv = __uint_as_float(pack_bf16(t1[n * D + d], 0.f) << 16);
            acc += __uint_as_float((u32)x[n * D + d] << 16) * __uint_as_float((u32)w[d * D + e] << 16)
                 + tv * __uint_as_float((u32)w[D * D + d * D + e] << 16);
        }
        out[i] = (unsigned short)pack_bf16(acc, 0.f);
    }
}

extern "C" void kernel_launch(void* const* d_in, const int* in_sizes, int n_in,
                              void* d_out, int out_size, void* d_ws, size_t ws_size,
                              hipStream_t stream) {
    const unsigned short* x = (const unsigned short*)d_in[0];
    const int* src = (const int*)d_in[1];
    const int* dst = (const int*)d_in[2];
    const unsigned short* ew = (const unsigned short*)d_in[3];
    const unsigned short* w = (const unsigned short*)d_in[4];
    unsigned short* out = (unsigned short*)d_out;
    const int N = in_sizes[0] / D;
    const int E = in_sizes[1];

    int npb = (N + MAX_BUCKETS - 1) / MAX_BUCKETS;
    if (npb < 1) npb = 1;
    const int nBuckets = (N + npb - 1) / npb;
    const u32 magic = npb == 1 ? 0xFFFFFFFFu : (u32)((0x100000000ull / (u64)npb) + 1ull);

    int chunk = (E + MAX_CHUNKS - 1) / MAX_CHUNKS;
    chunk = (chunk + 3) & ~3;
    if (chunk < 4) chunk = 4;
    int nChunks = (E + chunk - 1) / chunk;
    if (nChunks < 1) nChunks = 1;

    if (npb > MAX_NPB || chunk > A_CHUNK_MAX) {
        float* t1 = (float*)d_ws;
        fb_zero<<<2048, 256, 0, stream>>>(t1, (size_t)N * D);
        fb_scatter<<<4096, 256, 0, stream>>>(x, src, dst, ew, t1, (size_t)E);
        fb_gemm<<<4096, 256, 0, stream>>>(x, t1, w, out, (size_t)N);
        return;
    }

    char* ws = (char*)d_ws;
    unsigned short* wfrag = (unsigned short*)ws;
    u32* offT = (u32*)(ws + 16384);
    u64* recs = (u64*)(ws + 16384 + (size_t)MAX_BUCKETS * MAX_CHUNKS * 4);

    bin_kernel<<<nChunks + 1, A_THREADS, 0, stream>>>(src, dst, ew, w, E, chunk, npb, magic, nBuckets, nChunks,
                                                  (N + 3) / 4, recs, offT, wfrag);
    gather_kernel<<<nBuckets, B_THREADS, 0, stream>>>(x, recs, offT, (const uint4*)wfrag, out,
                                                      N, npb, nChunks, chunk);
}
```
